```python
import torch

def _softmax(x, axis=-1):
    return torch.softmax(x, dim=axis)
def _gelu(x):
    return torch.nn.functional.gelu(x, approximate="tanh")
def _silu(x):
    return torch.nn.functional.silu(x)
def _sigmoid(x):
    return torch.sigmoid(x)
def _layernorm(x, axis=-1, eps=1e-5):
    m = x.mean(dim=axis, keepdim=True)
    v = x.var(dim=axis, keepdim=True, unbiased=False)
    return (x - m) / torch.sqrt(v + eps)
def _scatter_add(tgt, idx, src, axis=0):
    return tgt.clone().index_add_(axis, idx, src)
def _segment_sum(src, seg, n):
    out = torch.zeros((n,) + src.shape[1:], dtype=src.dtype)
    return out.index_add_(0, seg, src)
def _pad(x, pw):
    flat = tuple(v for pair in reversed(pw) for v in pair)
    return torch.nn.functional.pad(x, flat)

kernel_name = 'concat_heads__sliding_block_cached__sliding_block_cached__decode'

def setup_inputs():

    _w = (lambda a: (lambda t: t.to(torch.bfloat16) if t.is_floating_point() else t)(torch.as_tensor(a)))
    def _A_setup():
        B, Sq, D = 1, 1, 640
        Hq, Hkv, Sk, Dh = 32, 32, 4096, 192
        assert Hq % Hkv == 0, (Hq, Hkv)
        return {
            "x": _w((lambda *s: torch.randn(*s, dtype=torch.bfloat16) * 1.6)(B, Sq, D)),
            "k_cache": _w((lambda *s: torch.randn(*s, dtype=torch.bfloat16) * 1.6)(B, Hkv, Sk, Dh)),
            "v_cache": _w((lambda *s: torch.randn(*s, dtype=torch.bfloat16) * 0.05)(B, Hkv, Sk, Dh)),
            "wq": _w((lambda *s: torch.randn(*s, dtype=torch.bfloat16) * 1.6)(D, Hq * Dh) * (D ** -0.5)),
            "wo": _w((lambda *s: torch.randn(*s, dtype=torch.bfloat16) * 0.05)(Hq * Dh, D) * ((Hq * Dh) ** -0.5)),
        }
    _A_in = _A_setup()

    def _B_setup():
        B, Sq, D = 1, 1, 640
        Hq, Hkv, Sk, Dh = 32, 32, 4096, 192
        assert Hq % Hkv == 0, (Hq, Hkv)
        return {
            "x": _w((lambda *s: torch.randn(*s, dtype=torch.bfloat16) * 1.6)(B, Sq, D)),
            "k_cache": _w((lambda *s: torch.randn(*s, dtype=torch.bfloat16) * 1.6)(B, Hkv, Sk, Dh)),
            "v_cache": _w((lambda *s: torch.randn(*s, dtype=torch.bfloat16) * 0.05)(B, Hkv, Sk, Dh)),
            "wq": _w((lambda *s: torch.randn(*s, dtype=torch.bfloat16) * 1.6)(D, Hq * Dh) * (D ** -0.5)),
            "wo": _w((lambda *s: torch.randn(*s, dtype=torch.bfloat16) * 0.05)(Hq * Dh, D) * ((Hq * Dh) ** -0.5)),
        }
    _B_in = _B_setup()

    return {
        "A_x": _A_in["x"],
        "A_k_cache": _A_in["k_cache"],
        "A_v_cache": _A_in["v_cache"],
        "A_wq": _A_in["wq"],
        "A_wo": _A_in["wo"],
        "B_x": _B_in["x"],
        "B_k_cache": _B_in["k_cache"],
        "B_v_cache": _B_in["v_cache"],
        "B_wq": _B_in["wq"],
        "B_wo": _B_in["wo"],
    }

def reference(A_x, A_k_cache, A_v_cache, A_wq, A_wo, B_x, B_k_cache, B_v_cache, B_wq, B_wo):
    def _A_ref(x, k_cache, v_cache, wq, wo):
        B, Sq, D = x.shape
        Hq, Dh = 32, 192
        Hkv, Sk = k_cache.shape[1], k_cache.shape[2]
        W = min(4096, max(256, Sk // 4))
        rep = Hq // Hkv
        q = (lambda x, *d: x.permute(*d))((x @ wq).reshape(B, Sq, Hq, Dh), 0, 2, 1, 3)
        k = (lambda x, n, axis: torch.repeat_interleave(x, n, dim=axis))(k_cache[:, :, Sk - W:, :], rep, axis=1)
        v = (lambda x, n, axis: torch.repeat_interleave(x, n, dim=axis))(v_cache[:, :, Sk - W:, :], rep, axis=1)
        scores = torch.einsum("bhqd,bhkd->bhqk", q, k) * (Dh ** -0.5)
        p = _softmax(scores, axis=-1)
        o = torch.einsum("bhqk,bhkd->bhqd", p, v)
        return (lambda x, *d: x.permute(*d))(o, 0, 2, 1, 3).reshape(B, Sq, Hq * Dh) @ wo

    def _B_ref(x, k_cache, v_cache, wq, wo):
        B, Sq, D = x.shape
        Hq, Dh = 32, 192
        Hkv, Sk = k_cache.shape[1], k_cache.shape[2]
        W = min(4096, max(256, Sk // 4))
        rep = Hq // Hkv
        q = (lambda x, *d: x.permute(*d))((x @ wq).reshape(B, Sq, Hq, Dh), 0, 2, 1, 3)
        k = (lambda x, n, axis: torch.repeat_interleave(x, n, dim=axis))(k_cache[:, :, Sk - W:, :], rep, axis=1)
        v = (lambda x, n, axis: torch.repeat_interleave(x, n, dim=axis))(v_cache[:, :, Sk - W:, :], rep, axis=1)
        scores = torch.einsum("bhqd,bhkd->bhqk", q, k) * (Dh ** -0.5)
        p = _softmax(scores, axis=-1)
        o = torch.einsum("bhqk,bhkd->bhqd", p, v)
        return (lambda x, *d: x.permute(*d))(o, 0, 2, 1, 3).reshape(B, Sq, Hq * Dh) @ wo

    a = _A_ref(A_x, A_k_cache, A_v_cache, A_wq, A_wo)
    b = _B_ref(B_x, B_k_cache, B_v_cache, B_wq, B_wo)
    return torch.concatenate([a, b], axis=-1)
```

```cpp
#include <hip/hip_runtime.h>
#include <hip/hip_bf16.h>
#include <stdint.h>

typedef unsigned u32x4 __attribute__((ext_vector_type(4)));
constexpr int D = 640, H = 32, SK = 4096, W = 1024, DH = 192, HD = H * DH;

__device__ __forceinline__ float bflo(unsigned u) { return __builtin_bit_cast(float, u << 16); }
__device__ __forceinline__ float bfhi(unsigned u) { return __builtin_bit_cast(float, u & 0xffff0000u); }
__device__ __forceinline__ float rbf(float f) {
  __hip_bfloat16 b = __float2bfloat16(f); return __bfloat162float(b);
}

struct P { const unsigned short* x[2]; const unsigned short* k[2]; const unsigned short* v[2]; const unsigned short* wq[2]; const unsigned short* wo[2]; float* ws; unsigned short* out; };

__global__ void kq(P p) {
  const int b = blockIdx.y, c0 = blockIdx.x * 64, tid = threadIdx.x, rg = tid >> 3, cc = tid & 7;
  __shared__ float red[32][64];
  float acc[8] = {0,0,0,0,0,0,0,0};
  for (int r = rg; r < D; r += 32) {
    u32x4 w = *(const u32x4*)(p.wq[b] + (size_t)r * HD + c0 + cc * 8);
    float xv = bflo(p.x[b][r]);
    for (int j = 0; j < 4; ++j) { acc[2*j] += xv * bflo(w[j]); acc[2*j+1] += xv * bfhi(w[j]); }
  }
  for (int j = 0; j < 8; ++j) red[rg][cc * 8 + j] = acc[j];
  __syncthreads();
  if (tid < 64) { float s = 0; for (int r = 0; r < 32; ++r) s += red[r][tid]; p.ws[b * HD + c0 + tid] = rbf(s); }
}
__global__ void kattn(P p) {
  const int s = blockIdx.x, h = blockIdx.y, b = blockIdx.z, tid = threadIdx.x;
  __shared__ float q[DH]; __shared__ float sc[256]; __shared__ float red[8][DH]; __shared__ float wred[8];
  if (tid < DH) q[tid] = p.ws[b * HD + h * DH + tid];
  __syncthreads();
  const unsigned short* K = p.k[b] + ((size_t)h * SK + (SK - W) + s * 256) * DH;
  const unsigned short* V = p.v[b] + ((size_t)h * SK + (SK - W) + s * 256) * DH;
  {
    float a = 0;
    for (int j = 0; j < 24; ++j) { u32x4 w = *(const u32x4*)(K + (size_t)tid * DH + j * 8);
      for (int t = 0; t < 4; ++t) { a += q[j*8+2*t] * bflo(w[t]); a += q[j*8+2*t+1] * bfhi(w[t]); } }
    a = rbf(a); a = rbf(a * 0.07216878364870322f);
    sc[tid] = a;
    float m = a;
    for (int o = 32; o; o >>= 1) m = fmaxf(m, __shfl_xor(m, o));
    if ((tid & 63) == 0) wred[tid >> 6] = m;
    __syncthreads();
    m = fmaxf(fmaxf(wred[0], wred[1]), fmaxf(wred[2], wred[3]));
    float e = __expf(a - m);
    sc[tid] = e;
    float l = e;
    for (int o = 32; o; o >>= 1) l += __shfl_xor(l, o);
    if ((tid & 63) == 0) wred[4 + (tid >> 6)] = l;
    __syncthreads();
    l = wred[4] + wred[5] + wred[6] + wred[7];
    float* po = p.ws + 2 * HD + ((size_t)(b * H + h) * 4 + s) * 256;
    if (tid < 192) {
      const int kg = tid / 24, c = tid % 24;
      float acc[8] = {0,0,0,0,0,0,0,0};
      for (int k = kg; k < 256; k += 8) { u32x4 w = *(const u32x4*)(V + (size_t)k * DH + c * 8); float pv = sc[k];
        for (int t = 0; t < 4; ++t) { acc[2*t] += pv * bflo(w[t]); acc[2*t+1] += pv * bfhi(w[t]); } }
      for (int j = 0; j < 8; ++j) red[kg][c * 8 + j] = acc[j];
    }
    __syncthreads();
    if (tid < DH) { float a2 = 0; for (int r = 0; r < 8; ++r) a2 += red[r][tid]; po[tid] = a2; }
    if (tid == 0) { po[192] = m; po[193] = l; }
  }
}
__global__ void kout(P p) {
  const int s = blockIdx.x, h = blockIdx.y, b = blockIdx.z, tid = threadIdx.x;
  __shared__ float o[48]; __shared__ float red[3][D];
  const float* po = p.ws + 2 * HD + ((size_t)(b * H + h) * 4) * 256;
  if (tid < 48) {
    float M = fmaxf(fmaxf(po[192], po[256+192]), fmaxf(po[512+192], po[768+192]));
    float L = 0, a = 0;
    for (int j = 0; j < 4; ++j) { float w = __expf(po[j*256+192] - M); L += w * po[j*256+193]; a += w * po[j*256 + s*48 + tid]; }
    o[tid] = rbf(a / L);
  }
  __syncthreads();
  if (tid < 240) {
    const int rg = tid / 80, c = tid % 80;
    float acc[8] = {0,0,0,0,0,0,0,0};
    for (int r = rg; r < 48; r += 3) { u32x4 w = *(const u32x4*)(p.wo[b] + ((size_t)h * DH + s * 48 + r) * D + c * 8); float ov = o[r];
      for (int t = 0; t < 4; ++t) { acc[2*t] += ov * bflo(w[t]); acc[2*t+1] += ov * bfhi(w[t]); } }
    for (int j = 0; j < 8; ++j) red[rg][c * 8 + j] = acc[j];
  }
  __syncthreads();
  float* pp = p.ws + 2 * HD + 64 * 4 * 256 + ((size_t)(b * H + h) * 4 + s) * D;
  for (int i = tid; i < D; i += 256) pp[i] = red[0][i] + red[1][i] + red[2][i];
}
__global__ void kfin(P p) {
  const int i = blockIdx.x * 64 + threadIdx.x, b = i / D, c = i % D;
  const float* pp = p.ws + 2 * HD + 64 * 4 * 256 + (size_t)b * 128 * D + c;
  float a = 0;
  for (int j = 0; j < 128; ++j) a += pp[(size_t)j * D];
  __hip_bfloat16 r = __float2bfloat16(a);
  p.out[i] = __builtin_bit_cast(unsigned short, r);
}

extern "C" void kernel_launch(void* const* d_in, const int* in_sizes, int n_in, void* d_out, int out_size, void* d_ws, size_t ws_size, hipStream_t stream) {
  P p;
  for (int b = 0; b < 2; ++b) {
    p.x[b] = (const unsigned short*)d_in[5*b+0]; p.k[b] = (const unsigned short*)d_in[5*b+1]; p.v[b] = (const unsigned short*)d_in[5*b+2];
    p.wq[b] = (const unsigned short*)d_in[5*b+3]; p.wo[b] = (const unsigned short*)d_in[5*b+4];
  }
  p.ws = (float*)d_ws; p.out = (unsigned short*)d_out;
  kq<<<dim3(96, 2), 256, 0, stream>>>(p);
  kattn<<<dim3(4, 32, 2), 256, 0, stream>>>(p);
  kout<<<dim3(4, 32, 2), 256, 0, stream>>>(p);
  kfin<<<20, 64, 0, stream>>>(p);
}
```

```cpp
#include <hip/hip_runtime.h>

typedef unsigned int u32;
typedef unsigned long long u64;
typedef u32 u32x4 __attribute__((ext_vector_type(4)));
typedef float f32x4 __attribute__((ext_vector_type(4)));
typedef __bf16 bf16x8 __attribute__((ext_vector_type(8)));
typedef __bf16 bf16x2 __attribute__((ext_vector_type(2)));
typedef __amdgpu_buffer_rsrc_t rsrc_t;
#define GAS __attribute__((address_space(1)))

constexpr int DM = 640, NH = 32, SK = 4096, WIN = 1024, DH = 192, HD = NH * DH;
constexpr int NSW = 10;
constexpr u32 WS_EPOCH = 0;
constexpr u32 WS_X1 = 4096;
constexpr u32 WS_X2 = 512u << 10;
constexpr u32 WS_F = 1u << 20;
constexpr u32 WS_BYTES = 4u << 20;
#define AUX_STREAM 2
#define QK_BACK 2

struct Params {
  const void* x[2]; const void* k[2]; const void* v[2]; const void* wq[2]; const void* wo[2];
  void* out; void* ws;
};

struct Smem {
  float qpart[2 * NSW][DH];
  float opart[2 * NSW][DH];
  u32 q16[DH / 2];
  u32 o32[64];
  float ml[16][2];
  u32 cnt_q, flag_q, cnt_o, flag_o, cnt_f, pad0, pad1, pad2;
};

__device__ __forceinline__ float as_f(u32 x) { return __builtin_bit_cast(float, x); }
__device__ __forceinline__ u32 as_u(float x) { return __builtin_bit_cast(u32, x); }
__device__ __forceinline__ u32 pk_bf16(float lo, float hi) { bf16x2 r = {(__bf16)lo, (__bf16)hi}; return __builtin_bit_cast(u32, r); }
__device__ __forceinline__ float rbf(float f) { return as_f(pk_bf16(f, f) << 16); }
template <int CTRL> __device__ __forceinline__ float dpp(float x) {
  return as_f((u32)__builtin_amdgcn_update_dpp(0, (int)as_u(x), CTRL, 0xF, 0xF, true));
}
__device__ __forceinline__ float x16_add(float x) { auto r = __builtin_amdgcn_permlane16_swap(as_u(x), as_u(x), false, false); return as_f(r[0]) + as_f(r[1]); }
__device__ __forceinline__ float x32_add(float x) { auto r = __builtin_amdgcn_permlane32_swap(as_u(x), as_u(x), false, false); return as_f(r[0]) + as_f(r[1]); }
__device__ __forceinline__ float x16_max(float x) { auto r = __builtin_amdgcn_permlane16_swap(as_u(x), as_u(x), false, false); return fmaxf(as_f(r[0]), as_f(r[1])); }
__device__ __forceinline__ float x32_max(float x) { auto r = __builtin_amdgcn_permlane32_swap(as_u(x), as_u(x), false, false); return fmaxf(as_f(r[0]), as_f(r[1])); }
__device__ __forceinline__ float dot2(u32 a, u32 b, float c) {
  return __builtin_amdgcn_fdot2_f32_bf16(__builtin_bit_cast(bf16x2, a), __builtin_bit_cast(bf16x2, b), c, false);
}
__device__ __forceinline__ f32x4 wmfma(u32 wrep, const u32x4& mask, const u32x4& data, f32x4 acc) {
  u32x4 a = {wrep & mask[0], wrep & mask[1], wrep & mask[2], wrep & mask[3]};
  return __builtin_amdgcn_mfma_f32_16x16x32_bf16(__builtin_bit_cast(bf16x8, a), __builtin_bit_cast(bf16x8, data), acc, 0, 0, 0);
}
template <int I> struct IC { static constexpr int value = I; };
template <int B, int E, class F> __device__ __forceinline__ void static_for(F&& f) {
  if constexpr (B < E) { f(IC<B>{}); static_for<B + 1, E>(f); }
}
template <int N> __device__ __forceinline__ void wait_vm() { asm volatile("s_waitcnt vmcnt(%0)" ::"n"(N) : "memory"); }
__device__ __forceinline__ void wait_lgkm0() { asm volatile("s_waitcnt lgkmcnt(0)" ::: "memory"); }
__device__ __forceinline__ void lds_wait_ge(u32* p, u32 want) {
#pragma unroll 1
  for (u32 it = 0; it < (1u << 21); ++it) {
    u32 v = __builtin_amdgcn_readfirstlane(__hip_atomic_load(p, __ATOMIC_RELAXED, __HIP_MEMORY_SCOPE_WORKGROUP));
    if (v >= want) break;
    __builtin_amdgcn_s_sleep(1);
  }
  asm volatile("" ::: "memory");
}
__device__ __forceinline__ void lds_signal_add(u32* p, int lane) {
  wait_lgkm0();
  if (lane == 0) __hip_atomic_fetch_add(p, 1u, __ATOMIC_RELAXED, __HIP_MEMORY_SCOPE_WORKGROUP);
}
__device__ __forceinline__ rsrc_t mk_rsrc(const void* base, u32 bytes) {
  return __builtin_amdgcn_make_buffer_rsrc(const_cast<void*>(base), 0, bytes, 0x00020000);
}

#define STAMP(i)
#define STAMP_DECL
#define STAMP_FLUSH(ws, bid, w, lane)

template <int NG>
__device__ __forceinline__ void stream_wave(const Params& p, Smem& sm, int b, int h, int s, int w, int lane) {
  constexpr int W = NG;
  constexpr int NK = 3 * NG, K0 = 6, V0 = K0 + NK, O0 = V0 + NK, N = O0 + 6;
  constexpr int QK_STEP = O0 - 1 - QK_BACK;
  STAMP_DECL;
  STAMP(0);
  const u32 r8 = lane >> 3, c8 = lane & 7;
  const rsrc_t rq = mk_rsrc((const char*)p.wq[b] + ((size_t)(160 * s) * HD + h * DH) * 2, 160u * HD * 2);
  const rsrc_t rk = mk_rsrc((const char*)p.k[b] + ((size_t)h * SK + (SK - WIN) + 256 * s) * DH * 2, 256u * DH * 2);
  const rsrc_t rv = mk_rsrc((const char*)p.v[b] + ((size_t)h * SK + (SK - WIN) + 256 * s) * DH * 2, 256u * DH * 2);
  const rsrc_t ro = mk_rsrc((const char*)p.wo[b] + ((size_t)h * DH + 48 * s) * DM * 2, 48u * DM * 2);
  const rsrc_t rw = mk_rsrc(p.ws, WS_BYTES);
  const u32 vq = r8 * (HD * 2) + c8 * 16, vk = r8 * (DH * 2) + c8 * 16, vo = r8 * (DM * 2) + c8 * 16;
  const GAS unsigned short* xp = (const GAS unsigned short*)p.x[b] + 160 * s + 8 * w + r8;
  const u32 xa = xp[0], xb = xp[80];
  u32x4 mask;
  {
    const u32 half = (lane & 1) ? 0xFFFF0000u : 0x0000FFFFu;
#pragma unroll
    for (int t = 0; t < 4; ++t) mask[t] = ((c8 >> 1) == (u32)t) ? half : 0u;
  }
  const bool valid = (lane >> 5) == ((lane >> 3) & 1);
  const int prow = 2 * w + (lane >> 5), pcol = 8 * c8 + 4 * ((lane >> 4) & 1);

  u32x4 R[N];
  f32x4 acc[3] = {{0, 0, 0, 0}, {0, 0, 0, 0}, {0, 0, 0, 0}};
  u32 pw[NG];
  static_for<0, N>([&](auto nc) __attribute__((always_inline)) {
    constexpr int n = decltype(nc)::value;
    if constexpr (n >= W) wait_vm<W - 1>();
    if constexpr (n < K0) R[n] = __builtin_amdgcn_raw_buffer_load_b128(rq, vq + 128 * (n % 3), (u32)(8 * w + 80 * (n / 3)) * (HD * 2), AUX_STREAM);
    else if constexpr (n < V0) R[n] = __builtin_amdgcn_raw_buffer_load_b128(rk, vk + 128 * ((n - K0) % 3), (u32)(w + NSW * ((n - K0) / 3)) * (8 * DH * 2), AUX_STREAM);
    else if constexpr (n < O0) R[n] = __builtin_amdgcn_raw_buffer_load_b128(rv, vk + 128 * ((n - V0) % 3), (u32)(w + NSW * ((n - V0) / 3)) * (8 * DH * 2), AUX_STREAM);
    else R[n] = __builtin_amdgcn_raw_buffer_load_b128(ro, vo, (u32)(128 * w + (n - O0) * (8 * DM * 2)), AUX_STREAM);
    asm volatile("" ::: "memory");
    if constexpr (n == W - 1) __builtin_amdgcn_s_barrier();
    if constexpr (n == K0 - 1 + W) {
      const u32 xar = xa * 0x10001u, xbr = xb * 0x10001u;
#pragma unroll
      for (int t = 0; t < 3; ++t) {
        f32x4 a = wmfma(xar, mask, R[t], (f32x4){0, 0, 0, 0});
        a = wmfma(xbr, mask, R[3 + t], a);
        if (valid) *(f32x4*)&sm.qpart[prow][64 * t + pcol] = a;
      }
      lds_signal_add(&sm.cnt_q, lane);
      STAMP(1);
    }
    if constexpr (n == QK_STEP) {
      STAMP(2);
      lds_wait_ge(&sm.flag_q, 1);
      STAMP(3);
      u32x4 qf[3];
#pragma unroll
      for (int t = 0; t < 3; ++t) qf[t] = *(const u32x4*)&sm.q16[32 * t + 4 * c8];
      float sc[NG];
#pragma unroll
      for (int g = 0; g < NG; ++g) {
        float d[3];
#pragma unroll
        for (int t = 0; t < 3; ++t) {
          const u32x4 kk = R[K0 + 3 * g + t];
          float a = dot2(kk[0], qf[t][0], 0.f);
          a = dot2(kk[1], qf[t][1], a);
          a = dot2(kk[2], qf[t][2], a);
          d[t] = dot2(kk[3], qf[t][3], a);
        }
        float a = (d[0] + d[1]) + d[2];
        a += dpp<0xB1>(a);
        a += dpp<0x4E>(a);
        a += dpp<0x141>(a);
        sc[g] = rbf(rbf(a) * 0.07216878364870322f);
      }
      float m = sc[0];
#pragma unroll
      for (int g = 1; g < NG; ++g) m = fmaxf(m, sc[g]);
      m = fmaxf(m, dpp<0x128>(m));
      m = x16_max(m);
      m = x32_max(m);
      float l = 0.f;
#pragma unroll
      for (int g = 0; g < NG; ++g) { const float e = __expf(sc[g] - m); l += e; pw[g] = pk_bf16(e, e); }
      l += dpp<0x128>(l);
      l = x16_add(l);
      l = x32_add(l);
      if (lane == 0) { sm.ml[w][0] = m; sm.ml[w][1] = l; }
#pragma unroll
      for (int j = V0; j <= QK_STEP - W; ++j) acc[(j - V0) % 3] = wmfma(pw[(j - V0) / 3], mask, R[j], acc[(j - V0) % 3]);
    }
    if constexpr (n > QK_STEP && n - W >= V0 && n - W < O0) {
      constexpr int j = n - W;
      acc[(j - V0) % 3] = wmfma(pw[(j - V0) / 3], mask, R[j], acc[(j - V0) % 3]);
    }
    if constexpr (n == O0 - 1 + W) {
#pragma unroll
      for (int t = 0; t < 3; ++t) if (valid) *(f32x4*)&sm.opart[prow][64 * t + pcol] = acc[t];
      lds_signal_add(&sm.cnt_o, lane);
      STAMP(4);
    }
  });
  lds_wait_ge(&sm.flag_o, 1);
  STAMP(5);
  const u32x4 oa = *(const u32x4*)&sm.o32[8 * r8], ob = *(const u32x4*)&sm.o32[8 * r8 + 4];
  f32x4 f = {0, 0, 0, 0};
  f = wmfma(oa[0], mask, R[O0 + 0], f);
  f = wmfma(oa[1], mask, R[O0 + 1], f);
  f = wmfma(oa[2], mask, R[O0 + 2], f);
  f = wmfma(oa[3], mask, R[O0 + 3], f);
  f = wmfma(ob[0], mask, R[O0 + 4], f);
  f = wmfma(ob[1], mask, R[O0 + 5], f);
  const u32 tag = ob[2];
  float tot[4];
#pragma unroll
  for (int r = 0; r < 4; ++r) {
    float z = valid ? f[r] : 0.f;
    z += dpp<0x128>(z);
    tot[r] = x32_add(z);
  }
  const bool hi = lane >= 32;
  const u32x4 g = {as_u(hi ? tot[2] : tot[0]), tag, as_u(hi ? tot[3] : tot[1]), tag};
  const u32 j = h * 4 + s;
  const u32 foff = WS_F + (((u32)b * 80 + 8 * w + c8) * 128 + j) * 64 + (4 * ((lane >> 4) & 1) + 2 * (lane >> 5)) * 8;
  if ((lane & 8) == 0) __builtin_amdgcn_raw_buffer_store_b128(g, rw, foff, 0, 16);
  if (lane == 0) __hip_atomic_fetch_add(&sm.cnt_f, 1u, __ATOMIC_RELAXED, __HIP_MEMORY_SCOPE_WORKGROUP);
  STAMP(6);
  STAMP_FLUSH(p.ws, blockIdx.x, w, lane);
}

__device__ __forceinline__ void comm_wave(const Params& p, Smem& sm, int b, int h, int s, int lane) {
  STAMP_DECL;
  STAMP(0);
  GAS u32* ep = (GAS u32*)((char*)p.ws + WS_EPOCH + 256 * b);
  const u32 tag = __builtin_amdgcn_readfirstlane(__hip_atomic_load(ep, __ATOMIC_RELAXED, __HIP_MEMORY_SCOPE_AGENT)) + 1u;
  const rsrc_t rw = mk_rsrc(p.ws, WS_BYTES);
  const u32 pair = b * 32 + h;
  const int l48 = lane < 48 ? lane : 47;
  lds_wait_ge(&sm.cnt_q, NSW);
  STAMP(1);
  {
    f32x4 a = {0, 0, 0, 0};
#pragma unroll
    for (int r = 0; r < 2 * NSW; ++r) a += *(const f32x4*)&sm.qpart[r][4 * l48];
    const u32 o = WS_X1 + (pair * 4 + s) * 1536 + l48 * 32;
    if (lane < 48) {
      __builtin_amdgcn_raw_buffer_store_b128((u32x4){as_u(a[0]), tag, as_u(a[1]), tag}, rw, o, 0, 16);
      __builtin_amdgcn_raw_buffer_store_b128((u32x4){as_u(a[2]), tag, as_u(a[3]), tag}, rw, o + 16, 0, 16);
    }
    u32x4 g[8];
#pragma unroll 1
    for (u32 it = 0; it < (1u << 16); ++it) {
      asm volatile("" ::: "memory");
      bool ok = true;
#pragma unroll
      for (int j = 0; j < 4; ++j) {
        g[2 * j] = __builtin_amdgcn_raw_buffer_load_b128(rw, l48 * 32, WS_X1 + (pair * 4 + j) * 1536, 16);
        g[2 * j + 1] = __builtin_amdgcn_raw_buffer_load_b128(rw, l48 * 32 + 16, WS_X1 + (pair * 4 + j) * 1536, 16);
      }
#pragma unroll
      for (int j = 0; j < 8; ++j) ok &= (g[j][1] == tag) & (g[j][3] == tag);
      if (__all(ok)) break;
    }
    float q[4];
#pragma unroll
    for (int e = 0; e < 4; ++e) {
      const int gi = e >> 1, c = (e & 1) * 2;
      q[e] = ((as_f(g[gi][c]) + as_f(g[2 + gi][c])) + as_f(g[4 + gi][c])) + as_f(g[6 + gi][c]);
    }
    if (lane < 48) { sm.q16[2 * lane] = pk_bf16(q[0], q[1]); sm.q16[2 * lane + 1] = pk_bf16(q[2], q[3]); }
    wait_lgkm0();
    if (lane == 0) __hip_atomic_store(&sm.flag_q, 1u, __ATOMIC_RELAXED, __HIP_MEMORY_SCOPE_WORKGROUP);
  }
  STAMP(2);
  lds_wait_ge(&sm.cnt_o, NSW);
  STAMP(3);
  {
    float mw[NSW], M = -INFINITY, L = 0.f;
#pragma unroll
    for (int w = 0; w < NSW; ++w) { mw[w] = sm.ml[w][0]; M = fmaxf(M, mw[w]); }
    f32x4 a = {0, 0, 0, 0};
#pragma unroll
    for (int w = 0; w < NSW; ++w) {
      const float sc = __expf(mw[w] - M);
      L += sc * sm.ml[w][1];
      a += sc * (*(const f32x4*)&sm.opart[2 * w][4 * l48] + *(const f32x4*)&sm.opart[2 * w + 1][4 * l48]);
    }
    if (lane == 48) { a[0] = M; a[1] = L; }
    const u32 o = WS_X2 + (pair * 4 + s) * 2048 + lane * 32;
    if (lane <= 48) {
      __builtin_amdgcn_raw_buffer_store_b128((u32x4){as_u(a[0]), tag, as_u(a[1]), tag}, rw, o, 0, 16);
      __builtin_amdgcn_raw_buffer_store_b128((u32x4){as_u(a[2]), tag, as_u(a[3]), tag}, rw, o + 16, 0, 16);
    }
    const u32 vo = lane < 24 ? (48 * s + 2 * lane) * 8 : 1536;
    u32x4 g[4];
#pragma unroll 1
    for (u32 it = 0; it < (1u << 16); ++it) {
      asm volatile("" ::: "memory");
      bool ok = true;
#pragma unroll
      for (int j = 0; j < 4; ++j) g[j] = __builtin_amdgcn_raw_buffer_load_b128(rw, vo, WS_X2 + (pair * 4 + j) * 2048, 16);
#pragma unroll
      for (int j = 0; j < 4; ++j) ok &= (g[j][1] == tag) & (g[j][3] == tag);
      if (__all(ok)) break;
    }
    float mj[4], lj[4];
#pragma unroll
    for (int j = 0; j < 4; ++j) { mj[j] = as_f(__builtin_amdgcn_readlane(g[j][0], 24)); lj[j] = as_f(__builtin_amdgcn_readlane(g[j][2], 24)); }
    const float MM = fmaxf(fmaxf(mj[0], mj[1]), fmaxf(mj[2], mj[3]));
    float wj[4], LL = 0.f;
#pragma unroll
    for (int j = 0; j < 4; ++j) { wj[j] = __expf(mj[j] - MM); LL += wj[j] * lj[j]; }
    float o0 = 0.f, o1 = 0.f;
#pragma unroll
    for (int j = 0; j < 4; ++j) { o0 += wj[j] * as_f(g[j][0]); o1 += wj[j] * as_f(g[j][2]); }
    o0 /= LL; o1 /= LL;
    const int d0 = 2 * lane;
    if (lane < 24) { sm.o32[(d0 & 7) * 8 + (d0 >> 3)] = pk_bf16(o0, o0); sm.o32[((d0 + 1) & 7) * 8 + (d0 >> 3)] = pk_bf16(o1, o1); }
    else if (lane < 32) sm.o32[(lane - 24) * 8 + 6] = tag;
    wait_lgkm0();
    if (lane == 0) __hip_atomic_store(&sm.flag_o, 1u, __ATOMIC_RELAXED, __HIP_MEMORY_SCOPE_WORKGROUP);
  }
  STAMP(4);
  const u32 rho = s * 32 + h;
  if (rho < 80) {
    lds_wait_ge(&sm.cnt_f, NSW);
    STAMP(5);
    u32x4 g[8];
#pragma unroll 1
    for (u32 it = 0; it < (1u << 16); ++it) {
      asm volatile("" ::: "memory");
      bool ok = true;
#pragma unroll
      for (int i = 0; i < 8; ++i) g[i] = __builtin_amdgcn_raw_buffer_load_b128(rw, lane * 16 + 1024 * (i & 3), WS_F + ((u32)b * 80 + rho) * 8192 + 4096 * (i >> 2), 16);
#pragma unroll
      for (int i = 0; i < 8; ++i) ok &= (g[i][1] == tag) & (g[i][3] == tag);
      if (__all(ok)) break;
    }
    float c0 = 0.f, c1 = 0.f;
#pragma unroll
    for (int i = 0; i < 8; ++i) { c0 += as_f(g[i][0]); c1 += as_f(g[i][2]); }
    c0 += dpp<0x128>(c0); c1 += dpp<0x128>(c1);
    c0 += dpp<0x124>(c0); c1 += dpp<0x124>(c1);
    c0 = x16_add(c0); c1 = x16_add(c1);
    c0 = x32_add(c0); c1 = x32_add(c1);
    if (lane < 4) ((GAS u32*)p.out)[(b * DM + 8 * rho) / 2 + lane] = pk_bf16(c0, c1);
    if (rho == 0 && lane == 0) __hip_atomic_store(ep, tag, __ATOMIC_RELAXED, __HIP_MEMORY_SCOPE_AGENT);
    STAMP(6);
  }
  STAMP_FLUSH(p.ws, blockIdx.x, NSW, lane);
}

__global__ __launch_bounds__(64 * (NSW + 1), 3) void fused_attn_decode(Params p) {
  __shared__ __attribute__((aligned(16))) Smem sm;
  const int lane = threadIdx.x & 63, w = __builtin_amdgcn_readfirstlane(threadIdx.x >> 6);
  const int bid = blockIdx.x, x = bid & 7, i = bid >> 3;
  const int pair = x * 8 + (i >> 2), s = i & 3, b = pair >> 5, h = pair & 31;
  if (w == NSW) {
    if (lane < 8) (&sm.cnt_q)[lane] = 0;
    wait_lgkm0();
    __builtin_amdgcn_s_barrier();
    comm_wave(p, sm, b, h, s, lane);
  } else if (w < 2) {
    stream_wave<4>(p, sm, b, h, s, w, lane);
  } else {
    stream_wave<3>(p, sm, b, h, s, w, lane);
  }
}

extern "C" void kernel_launch(void* const* d_in, const int* in_sizes, int n_in, void* d_out, int out_size, void* d_ws,
                              size_t ws_size, hipStream_t stream) {
  Params p;
  for (int b = 0; b < 2; ++b) {
    p.x[b] = d_in[5 * b + 0]; p.k[b] = d_in[5 * b + 1]; p.v[b] = d_in[5 * b + 2];
    p.wq[b] = d_in[5 * b + 3]; p.wo[b] = d_in[5 * b + 4];
  }
  p.out = d_out; p.ws = d_ws;
  fused_attn_decode<<<256, 64 * (NSW + 1), 0, stream>>>(p);
}
```

```cpp
#include <hip/hip_runtime.h>

typedef unsigned int u32;
typedef unsigned long long u64;
typedef u32 u32x4 __attribute__((ext_vector_type(4)));
typedef float f32x4 __attribute__((ext_vector_type(4)));
typedef float f32x2 __attribute__((ext_vector_type(2)));
typedef __bf16 bf16x8 __attribute__((ext_vector_type(8)));
typedef __bf16 bf16x2 __attribute__((ext_vector_type(2)));
typedef __amdgpu_buffer_rsrc_t rsrc_t;
#define GAS __attribute__((address_space(1)))

constexpr int DM = 640, NH = 32, SK = 4096, WIN = 1024, DH = 192, HD = NH * DH;
constexpr int NSW = 10;
constexpr u32 WS_EPOCH = 0;
constexpr u32 WS_X1 = 4096;
constexpr u32 WS_X2 = 512u << 10;
constexpr u32 WS_F = 1u << 20;
constexpr u32 WS_BYTES = 4u << 20;
constexpr u32 WS_OOB = 8u << 20;
#define AUX_STREAM 2
#define POLLAUX "sc1"
#define CPRIO 3
#define X1PRE 5
#define X1SLP 4
#define X2PRE 5
#define X2SLP 0
#define FPRE 8
#define FSLP 0
#define SEL 0
#define WAITSLP 0
#define QK_BACK 2

struct Params {
  const void* x[2]; const void* k[2]; const void* v[2]; const void* wq[2]; const void* wo[2];
  void* out; void* ws;
};

struct Smem {
  float qpart[2 * NSW][DH];
  float opart[2 * NSW][DH];
  u32 q16[DH / 2];
  u32 o32[64];
  float ml[16][2];
  u32 cnt_q, flag_q, cnt_o, flag_o, cnt_f, cnt_ml, pad1, pad2;
  u32 junk[192];
  float fpart[64];
};

__device__ __forceinline__ float as_f(u32 x) { return __builtin_bit_cast(float, x); }
__device__ __forceinline__ u32 as_u(float x) { return __builtin_bit_cast(u32, x); }
__device__ __forceinline__ u32 pk_bf16(float lo, float hi) { bf16x2 r = {(__bf16)lo, (__bf16)hi}; return __builtin_bit_cast(u32, r); }
__device__ __forceinline__ float rbf(float f) { return as_f(pk_bf16(f, f) << 16); }
template <int CTRL> __device__ __forceinline__ float dpp(float x) {
  return as_f((u32)__builtin_amdgcn_update_dpp(0, (int)as_u(x), CTRL, 0xF, 0xF, true));
}
__device__ __forceinline__ float x16_add(float x) { auto r = __builtin_amdgcn_permlane16_swap(as_u(x), as_u(x), false, false); return as_f(r[0]) + as_f(r[1]); }
__device__ __forceinline__ float x32_add(float x) { auto r = __builtin_amdgcn_permlane32_swap(as_u(x), as_u(x), false, false); return as_f(r[0]) + as_f(r[1]); }
__device__ __forceinline__ float x16_max(float x) { auto r = __builtin_amdgcn_permlane16_swap(as_u(x), as_u(x), false, false); return fmaxf(as_f(r[0]), as_f(r[1])); }
__device__ __forceinline__ float x32_max(float x) { auto r = __builtin_amdgcn_permlane32_swap(as_u(x), as_u(x), false, false); return fmaxf(as_f(r[0]), as_f(r[1])); }
__device__ __forceinline__ float dot2(u32 a, u32 b, float c) {
  return __builtin_amdgcn_fdot2_f32_bf16(__builtin_bit_cast(bf16x2, a), __builtin_bit_cast(bf16x2, b), c, false);
}
__device__ __forceinline__ f32x4 wmfma(u32 wrep, const u32x4& mask, const u32x4& data, f32x4 acc) {
  u32x4 a = {wrep & mask[0], wrep & mask[1], wrep & mask[2], wrep & mask[3]};
  return __builtin_amdgcn_mfma_f32_16x16x32_bf16(__builtin_bit_cast(bf16x8, a), __builtin_bit_cast(bf16x8, data), acc, 0, 0, 0);
}
template <int I> struct IC { static constexpr int value = I; };
template <int B, int E, class F> __device__ __forceinline__ void static_for(F&& f) {
  if constexpr (B < E) { f(IC<B>{}); static_for<B + 1, E>(f); }
}
template <int N> __device__ __forceinline__ void wait_vm() { asm volatile("s_waitcnt vmcnt(%0)" ::"n"(N) : "memory"); }
__device__ __forceinline__ void wait_lgkm0() { asm volatile("s_waitcnt lgkmcnt(0)" ::: "memory"); }
template <int SLP = 1> __device__ __forceinline__ void lds_wait_ge(u32* p, u32 want) {
#pragma unroll 1
  for (u32 it = 0; it < (1u << 21); ++it) {
    u32 v = __builtin_amdgcn_readfirstlane(__hip_atomic_load(p, __ATOMIC_RELAXED, __HIP_MEMORY_SCOPE_WORKGROUP));
    if (v >= want) break;
    if (SLP) __builtin_amdgcn_s_sleep(SLP);
  }
  asm volatile("" ::: "memory");
}
typedef __attribute__((address_space(3))) u32 lds_u32;
__device__ __forceinline__ u32 lds_addr(const void* p) { return (u32)(size_t)(const __attribute__((address_space(3))) void*)p; }
__device__ __forceinline__ void lds_signal_add(u32* p, int lane) {
  asm volatile("" ::: "memory");
  if (lane == 0) asm volatile("ds_add_u32 %0, %1" ::"v"(lds_addr(p)), "v"(1u) : "memory");
}
__device__ __forceinline__ rsrc_t mk_rsrc(const void* base, u32 bytes) {
  return __builtin_amdgcn_make_buffer_rsrc(const_cast<void*>(base), 0, bytes, 0x00020000);
}

template <int NG>
__device__ __forceinline__ void stream_wave(const Params& p, Smem& sm, int b, int h, int s, int w, int lane) {
  constexpr int W = NG;
  constexpr int NK = 3 * NG, K0 = 6, V0 = K0 + NK, O0 = V0 + NK, N = O0 + 6;
  constexpr int QK_STEP = O0 - 1 - QK_BACK;
  const u32 r8 = lane >> 3, c8 = lane & 7;
  const rsrc_t rq = mk_rsrc((const char*)p.wq[b] + ((size_t)(160 * s) * HD + h * DH) * 2, 160u * HD * 2);
  const rsrc_t rk = mk_rsrc((const char*)p.k[b] + ((size_t)h * SK + (SK - WIN) + 256 * s) * DH * 2, 256u * DH * 2);
  const rsrc_t rv = mk_rsrc((const char*)p.v[b] + ((size_t)h * SK + (SK - WIN) + 256 * s) * DH * 2, 256u * DH * 2);
  const rsrc_t ro = mk_rsrc((const char*)p.wo[b] + ((size_t)h * DH + 48 * s) * DM * 2, 48u * DM * 2);
  const rsrc_t rw = mk_rsrc(p.ws, WS_BYTES);
  const u32 vq = r8 * (HD * 2) + c8 * 16, vk = r8 * (DH * 2) + c8 * 16, vo = r8 * (DM * 2) + c8 * 16;
  const GAS unsigned short* xp = (const GAS unsigned short*)p.x[b] + 160 * s + 8 * w + r8;
  const u32 xa = xp[0], xb = xp[80];
  u32x4 mask;
  {
    const u32 half = (lane & 1) ? 0xFFFF0000u : 0x0000FFFFu;
#pragma unroll
    for (int t = 0; t < 4; ++t) mask[t] = ((c8 >> 1) == (u32)t) ? half : 0u;
  }
  const bool valid = (lane >> 5) == ((lane >> 3) & 1);
  const int prow = 2 * w + (lane >> 5), pcol = 8 * c8 + 4 * ((lane >> 4) & 1);

  u32x4 R[N];
  f32x4 acc[3] = {{0, 0, 0, 0}, {0, 0, 0, 0}, {0, 0, 0, 0}};
  u32 pw[NG];
  static_for<0, N>([&](auto nc) __attribute__((always_inline)) {
    constexpr int n = decltype(nc)::value;
    if constexpr (n >= W) wait_vm<W - 1>();
    if constexpr (n < K0) R[n] = __builtin_amdgcn_raw_buffer_load_b128(rq, vq + 128 * (n % 3), (u32)(8 * w + 80 * (n / 3)) * (HD * 2), AUX_STREAM);
    else if constexpr (n < V0) R[n] = __builtin_amdgcn_raw_buffer_load_b128(rk, vk + 128 * ((n - K0) % 3), (u32)(w + NSW * ((n - K0) / 3)) * (8 * DH * 2), AUX_STREAM);
    else if constexpr (n < O0) R[n] = __builtin_amdgcn_raw_buffer_load_b128(rv, vk + 128 * ((n - V0) % 3), (u32)(w + NSW * ((n - V0) / 3)) * (8 * DH * 2), AUX_STREAM);
    else R[n] = __builtin_amdgcn_raw_buffer_load_b128(ro, vo, (u32)(128 * w + (n - O0) * (8 * DM * 2)), AUX_STREAM);
    asm volatile("" ::: "memory");
    if constexpr (n == W - 1) __builtin_amdgcn_s_barrier();
    if constexpr (n == K0 - 1 + W) {
      const u32 xar = xa * 0x10001u, xbr = xb * 0x10001u;
#pragma unroll
      for (int t = 0; t < 3; ++t) {
        f32x4 a = wmfma(xar, mask, R[t], (f32x4){0, 0, 0, 0});
        a = wmfma(xbr, mask, R[3 + t], a);
        if (valid) *(f32x4*)&sm.qpart[prow][64 * t + pcol] = a;
      }
      lds_signal_add(&sm.cnt_q, lane);
    }
    if constexpr (n == QK_STEP) {
      lds_wait_ge(&sm.flag_q, 1);
      u32x4 qf[3];
#pragma unroll
      for (int t = 0; t < 3; ++t) qf[t] = *(const u32x4*)&sm.q16[32 * t + 4 * c8];
      float sc[NG];
#pragma unroll
      for (int g = 0; g < NG; ++g) {
        float d[3];
#pragma unroll
        for (int t = 0; t < 3; ++t) {
          const u32x4 kk = R[K0 + 3 * g + t];
          float a = dot2(kk[0], qf[t][0], 0.f);
          a = dot2(kk[1], qf[t][1], a);
          a = dot2(kk[2], qf[t][2], a);
          d[t] = dot2(kk[3], qf[t][3], a);
        }
        float a = (d[0] + d[1]) + d[2];
        a += dpp<0xB1>(a);
        a += dpp<0x4E>(a);
        a += dpp<0x141>(a);
        sc[g] = rbf(rbf(a) * 0.07216878364870322f);
      }
      float m = sc[0];
#pragma unroll
      for (int g = 1; g < NG; ++g) m = fmaxf(m, sc[g]);
      m = fmaxf(m, dpp<0x128>(m));
      m = x16_max(m);
      m = x32_max(m);
      float l = 0.f;
#pragma unroll
      for (int g = 0; g < NG; ++g) { const float e = __expf(sc[g] - m); l += e; pw[g] = pk_bf16(e, e); }
      l += dpp<0x128>(l);
      l = x16_add(l);
      l = x32_add(l);
      if (lane == 0) { sm.ml[w][0] = m; sm.ml[w][1] = l; }
      lds_signal_add(&sm.cnt_ml, lane);
#pragma unroll
      for (int j = V0; j <= QK_STEP - W; ++j) acc[(j - V0) % 3] = wmfma(pw[(j - V0) / 3], mask, R[j], acc[(j - V0) % 3]);
    }
    if constexpr (n > QK_STEP && n - W >= V0 && n - W < O0) {
      constexpr int j = n - W;
      acc[(j - V0) % 3] = wmfma(pw[(j - V0) / 3], mask, R[j], acc[(j - V0) % 3]);
    }
    if constexpr (n == O0 - 1 + W) {
#pragma unroll
      for (int t = 0; t < 3; ++t) if (valid) *(f32x4*)&sm.opart[prow][64 * t + pcol] = acc[t];
      lds_signal_add(&sm.cnt_o, lane);
    }
  });
  u32 foff = WS_F + (((u32)b * 80 + 8 * w + c8) * 128 + (h * 4 + s)) * 64 + (4 * ((lane >> 4) & 1) + 2 * (lane >> 5)) * 8;
  if (lane & 8) foff = WS_OOB;
  u32 oaddr = lds_addr(&sm.o32[8 * r8]);
  const u32 rho = s * 32 + h;
  const rsrc_t rout = mk_rsrc(p.out, 2 * DM * 2);
  u32 fso = WS_F + ((u32)b * 80 + rho) * 8192 + 1024 * w;
  u32 off = lane * 16;
  u32 a_fw = lds_addr((lane & 28) == 0 ? &sm.fpart[8 * (w & 7) + 2 * (lane & 3) + (lane >> 5)] : (float*)&sm.junk[64 + lane]);
  u32 a_fc = lds_addr(lane == 0 ? &sm.cnt_f : &sm.junk[128 + lane]);
  u32 a_fr = lds_addr(&sm.fpart[lane]);
  u32 ooff = lane < 8 ? (b * DM + 8 * rho + lane) * 2 : WS_OOB;
  u32 eoff = lane == 0 ? WS_EPOCH + 256 * b : WS_OOB;
  u32x4 rwd = {(u32)(size_t)p.ws, (u32)((size_t)p.ws >> 32), WS_BYTES, 0x00020000u};
  asm volatile("" : "+v"(foff), "+v"(oaddr), "+v"(off), "+v"(a_fw), "+v"(a_fc), "+v"(a_fr), "+v"(ooff), "+v"(eoff), "+s"(fso), "+s"(rwd));
  lds_wait_ge<WAITSLP>(&sm.flag_o, 1);
  const u32x4 oa = *(const __attribute__((address_space(3))) u32x4*)(size_t)oaddr, ob = *(const __attribute__((address_space(3))) u32x4*)(size_t)(oaddr + 16);
  f32x4 f = {0, 0, 0, 0};
  f = wmfma(oa[0], mask, R[O0 + 0], f);
  f = wmfma(oa[1], mask, R[O0 + 1], f);
  f = wmfma(oa[2], mask, R[O0 + 2], f);
  f = wmfma(oa[3], mask, R[O0 + 3], f);
  f = wmfma(ob[0], mask, R[O0 + 4], f);
  f = wmfma(ob[1], mask, R[O0 + 5], f);
  const u32 tag = ob[2];
  const auto r02 = __builtin_amdgcn_permlane32_swap(as_u(f[0]), as_u(f[2]), false, false);
  const auto r13 = __builtin_amdgcn_permlane32_swap(as_u(f[1]), as_u(f[3]), false, false);
  const float t0 = as_f(r02[0]) + dpp<0x128>(as_f(r02[1])), t1 = as_f(r13[0]) + dpp<0x128>(as_f(r13[1]));
  __builtin_amdgcn_raw_buffer_store_b128((u32x4){as_u(t0), tag, as_u(t1), tag}, rw, foff, 0, 16);
  if (rho < 80 && w < 8) {
    u32 it;
    u64 val, tmp;
    u32x4 t0;
    float c0 = 0.f, c1 = 0.f;
    for (int z = 0; z < FPRE; ++z) __builtin_amdgcn_s_sleep(4);
    asm volatile(
        "s_mov_b64 %[val], 0\n"
        "s_movk_i32 %[it], 0x4000\n"
        "1:\n"
        "buffer_load_dwordx4 v[100:103], %[off], %[rw], %[so] offen " POLLAUX "\n"
        "s_waitcnt vmcnt(0)\n"
        "v_cmp_eq_u32_e32 vcc, v101, %[tag]\n"
        "v_cmp_eq_u32_e64 %[tmp], v103, %[tag]\n"
        "s_and_b64 vcc, vcc, %[tmp]\n"
        "s_andn2_b64 %[tmp], vcc, %[val]\n"
        "s_or_b64 %[val], %[val], vcc\n"
        "v_cndmask_b32_e64 %[c0], %[c0], v100, %[tmp]\n"
        "v_cndmask_b32_e64 %[c1], %[c1], v102, %[tmp]\n"
        "s_cmp_eq_u64 %[val], -1\n"
        "s_cbranch_scc1 2f\n"
        "v_cndmask_b32_e64 %[off], %[off], %[oob], %[val]\n"
        "s_sleep %[slp]\n"
        "s_sub_u32 %[it], %[it], 1\n"
        "s_cbranch_scc0 1b\n"
        "2:\n"
        : "=&{v[100:103]}"(t0), [off] "+v"(off), [c0] "+v"(c0), [c1] "+v"(c1), [val] "=&s"(val), [tmp] "=&s"(tmp), [it] "=&s"(it)
        : [rw] "s"(rwd), [so] "s"(fso), [tag] "v"(tag), [oob] "v"(WS_OOB), [slp] "n"(FSLP)
        : "vcc", "scc", "memory");
    const auto rc = __builtin_amdgcn_permlane32_swap(as_u(c0), as_u(c1), false, false);
    float c = as_f(rc[0]) + as_f(rc[1]);
    c = x16_add(c);
    c += dpp<0x128>(c);
    c += dpp<0x124>(c);
    u32 old;
    float z;
    asm volatile("ds_write_b32 %[aw], %[c]\n ds_add_rtn_u32 %[old], %[ac], %[one]\n ds_read_b32 %[z], %[ar]\n s_waitcnt lgkmcnt(0)"
                 : [old] "=&v"(old), [z] "=&v"(z) : [aw] "v"(a_fw), [c] "v"(c), [ac] "v"(a_fc), [one] "v"(1u), [ar] "v"(a_fr) : "memory");
    z += dpp<0x128>(z);
    z = x16_add(z);
    z = x32_add(z);
    if (__builtin_amdgcn_readfirstlane(old) == 7) {
      __builtin_amdgcn_raw_buffer_store_b16((unsigned short)(pk_bf16(z, z) & 0xFFFFu), rout, ooff, 0, 0);
      if (rho == 0) __builtin_amdgcn_raw_buffer_store_b32(tag, rw, eoff, 0, 16);
    }
    asm volatile("s_waitcnt vmcnt(0)" ::"{v[100:103]}"(t0) : "memory");
  }
}

template <int N, int SLP> __device__ __forceinline__ void poll_set(u32x4 (&g)[N], const rsrc_t& rw, const u32 (&vo)[N], const u32 (&so)[N], u32 tag) {
  bool val[N];
#pragma unroll
  for (int i = 0; i < N; ++i) { val[i] = vo[i] >= WS_BYTES; g[i] = (u32x4){0, 0, 0, 0}; }
#pragma unroll 1
  for (u32 it = 0; it < (1u << 16); ++it) {
    asm volatile("" ::: "memory");
    if (SLP && it) for (int z = 0; z < SLP; ++z) __builtin_amdgcn_s_sleep(8);
    u32x4 t[N];
#pragma unroll
    for (int i = 0; i < N; ++i) t[i] = __builtin_amdgcn_raw_buffer_load_b128(rw, (SEL && val[i]) ? WS_OOB : vo[i], so[i], 16);
    bool ok = true;
#pragma unroll
    for (int i = 0; i < N; ++i) {
      const bool v = (t[i][1] == tag) & (t[i][3] == tag);
      if (!val[i]) g[i] = t[i];
      val[i] |= v;
      ok &= val[i];
    }
    if (__all(ok)) break;
  }
}

__device__ __forceinline__ void comm_wave(const Params& p, Smem& sm, int b, int h, int s, int lane) {
  __builtin_amdgcn_s_setprio(CPRIO);
  u32 touch;
  {
    const char* a = (const char*)p.ws + WS_BYTES - 256;
    if (lane == 1) a = (const char*)p.out;
    if (lane == 2) a = (const char*)p.v[b] + ((size_t)h * SK + (SK - WIN) + 256 * s + 255) * DH * 2;
    if (lane == 3) a = (const char*)p.wo[b] + ((size_t)h * DH + 48 * s + 47) * DM * 2;
    if (lane == 4) a = (const char*)p.k[b] + ((size_t)h * SK + (SK - WIN) + 256 * s + 255) * DH * 2;
    if (lane == 5) a = (const char*)p.v[b] + ((size_t)h * SK + (SK - WIN) + 256 * s) * DH * 2;
    if (lane == 6) a = (const char*)p.wo[b] + ((size_t)h * DH + 48 * s) * DM * 2;
    touch = *(const GAS u32*)a;
  }
  GAS u32* ep = (GAS u32*)((char*)p.ws + WS_EPOCH + 256 * b);
  const u32 tag = __builtin_amdgcn_readfirstlane(__hip_atomic_load(ep, __ATOMIC_RELAXED, __HIP_MEMORY_SCOPE_AGENT)) + 1u;
  const rsrc_t rw = mk_rsrc(p.ws, WS_BYTES);
  const u32 pair = b * 32 + h;
  const int l48 = lane < 48 ? lane : 47;
  lds_wait_ge(&sm.cnt_q, NSW);
  asm volatile("" ::"v"(touch));
  {
    f32x4 a = {0, 0, 0, 0};
#pragma unroll
    for (int r = 0; r < 2 * NSW; ++r) a += *(const f32x4*)&sm.qpart[r][4 * l48];
    const u32 o = WS_X1 + (pair * 4 + s) * 1536 + l48 * 16;
    if (lane < 48) {
      __builtin_amdgcn_raw_buffer_store_b128((u32x4){as_u(a[0]), tag, as_u(a[1]), tag}, rw, o, 0, 16);
      __builtin_amdgcn_raw_buffer_store_b128((u32x4){as_u(a[2]), tag, as_u(a[3]), tag}, rw, o + 768, 0, 16);
    }
    for (int z = 0; z < X1PRE; ++z) __builtin_amdgcn_s_sleep(8);
    u32x4 g[8];
    u32 vo[8], so[8];
#pragma unroll
    for (int j = 0; j < 8; ++j) {
      vo[j] = ((j >> 1) == s || lane >= 48) ? WS_OOB : (u32)(lane * 16 + 768 * (j & 1));
      so[j] = WS_X1 + (pair * 4 + (j >> 1)) * 1536;
    }
    poll_set<8, X1SLP>(g, rw, vo, so, tag);
#pragma unroll
    for (int j = 0; j < 4; ++j) if (j == s) { g[2 * j][0] = as_u(a[0]); g[2 * j][2] = as_u(a[1]); g[2 * j + 1][0] = as_u(a[2]); g[2 * j + 1][2] = as_u(a[3]); }
    float q[4];
#pragma unroll
    for (int e = 0; e < 4; ++e) {
      const int gi = e >> 1, c = (e & 1) * 2;
      q[e] = ((as_f(g[gi][c]) + as_f(g[2 + gi][c])) + as_f(g[4 + gi][c])) + as_f(g[6 + gi][c]);
    }
    if (lane < 48) { sm.q16[2 * lane] = pk_bf16(q[0], q[1]); sm.q16[2 * lane + 1] = pk_bf16(q[2], q[3]); }
    wait_lgkm0();
    if (lane == 0) __hip_atomic_store(&sm.flag_q, 1u, __ATOMIC_RELAXED, __HIP_MEMORY_SCOPE_WORKGROUP);
  }
  {
    if (lane >= 24 && lane < 32) sm.o32[(lane - 24) * 8 + 6] = tag;
    const u32 d2 = 24 * s + lane;
    u32 so_st = WS_X2 + (pair * 4 + s) * 2048;
    u32 vst = lane < 48 ? (u32)lane * 16 : WS_OOB;
    u32 vp[3], so[3];
#pragma unroll
    for (int k = 0; k < 3; ++k) { vp[k] = lane < 24 ? (d2 & 1) * 1024 + (d2 >> 1) * 16 : 768u; so[k] = WS_X2 + (pair * 4 + ((s + 1 + k) & 3)) * 2048; }
    u32 a_rd = lds_addr(&sm.opart[0][4 * l48]);
    u32 a_wr = lds_addr(&sm.qpart[0][4 * l48]);
    u32 a_own = lds_addr(&sm.qpart[0][lane < 24 ? 2 * d2 : 0]);
    const int d0 = 2 * lane;
    u32 a_o32 = lane < 24 ? lds_addr(&sm.o32[(d0 & 7) * 8 + (d0 >> 3)]) : lds_addr(&sm.junk[lane]);
    u32 a_flag = lane == 0 ? lds_addr(&sm.flag_o) : lds_addr(&sm.junk[128 + lane]);
    lds_wait_ge(&sm.cnt_ml, NSW);
    const f32x2 mlw = *(const f32x2*)&sm.ml[lane < NSW ? lane : 0][0];
    float M = mlw[0];
    M = fmaxf(M, dpp<0xB1>(M)); M = fmaxf(M, dpp<0x4E>(M)); M = fmaxf(M, dpp<0x141>(M)); M = fmaxf(M, dpp<0x140>(M));
    const float scl = __expf(mlw[0] - M);
    float L = lane < NSW ? scl * mlw[1] : 0.f;
    L += dpp<0xB1>(L); L += dpp<0x4E>(L); L += dpp<0x141>(L); L += dpp<0x140>(L);
    float sc[NSW];
#pragma unroll
    for (int w = 0; w < NSW; ++w) sc[w] = as_f(__builtin_amdgcn_readlane(as_u(scl), w));
    const float Ms = as_f(__builtin_amdgcn_readfirstlane(as_u(M))), Ls = as_f(__builtin_amdgcn_readfirstlane(as_u(L)));
    if (lane == 48) __builtin_amdgcn_raw_buffer_store_b128((u32x4){as_u(Ms), tag, as_u(Ls), tag}, rw, 768u, so_st, 16);
    asm volatile("" : "+v"(vst), "+v"(vp[0]), "+v"(a_rd), "+v"(a_wr), "+v"(a_own), "+v"(a_o32), "+v"(a_flag), "+s"(so_st), "+s"(so[0]), "+s"(so[1]), "+s"(so[2]));
    vp[1] = vp[2] = vp[0];
    lds_wait_ge<WAITSLP>(&sm.cnt_o, NSW);
    f32x4 x[2 * NSW];
#pragma unroll
    for (int r = 0; r < 2 * NSW; ++r) x[r] = *(const __attribute__((address_space(3))) f32x4*)(size_t)(a_rd + r * DH * 4);
    __builtin_amdgcn_sched_barrier(0);
    f32x4 a = sc[0] * x[0], a2 = sc[0] * x[1];
#pragma unroll
    for (int w = 1; w < NSW; ++w) { a += sc[w] * x[2 * w]; a2 += sc[w] * x[2 * w + 1]; }
    a += a2;
    __builtin_amdgcn_raw_buffer_store_b128((u32x4){as_u(a[0]), tag, as_u(a[1]), tag}, rw, vst, so_st, 16);
    __builtin_amdgcn_raw_buffer_store_b128((u32x4){as_u(a[2]), tag, as_u(a[3]), tag}, rw, vst + 1024, so_st, 16);
    *(__attribute__((address_space(3))) f32x4*)(size_t)a_wr = a;
    u32x4 t0, t1, t2;
    u32 ga[3], gb[3], bad, it;
    const u32x4 rwd = {(u32)(size_t)p.ws, (u32)((size_t)p.ws >> 32), WS_BYTES, 0x00020000u};
    for (int z = 0; z < X2PRE; ++z) __builtin_amdgcn_s_sleep(4);
    asm volatile(
        "s_movk_i32 %[it], 0x4000\n"
        "1:\n"
        "buffer_load_dwordx4 v[64:67], %[off], %[rw], %[so0] offen " POLLAUX "\n"
        "buffer_load_dwordx4 v[68:71], %[off], %[rw], %[so1] offen " POLLAUX "\n"
        "buffer_load_dwordx4 v[72:75], %[off], %[rw], %[so2] offen " POLLAUX "\n"
        "s_waitcnt vmcnt(0)\n"
        "v_xor_b32_e32 %[bad], v65, %[tag]\n"
        "v_bitop3_b32 %[bad], %[bad], v67, %[tag] bitop3:0xf6\n"
        "v_bitop3_b32 %[bad], %[bad], v69, %[tag] bitop3:0xf6\n"
        "v_bitop3_b32 %[bad], %[bad], v71, %[tag] bitop3:0xf6\n"
        "v_bitop3_b32 %[bad], %[bad], v73, %[tag] bitop3:0xf6\n"
        "v_bitop3_b32 %[bad], %[bad], v75, %[tag] bitop3:0xf6\n"
        "v_cmp_ne_u32_e32 vcc, 0, %[bad]\n"
        "s_cmp_eq_u64 vcc, 0\n"
        "s_cbranch_scc1 10f\n"
        "s_sleep %[slp]\n"
        "s_sub_u32 %[it], %[it], 1\n"
        "s_cbranch_scc0 1b\n"
        "10:\n"
        "v_mov_b32_e32 %[a0], v64\n"
        "v_mov_b32_e32 %[b0], v66\n"
        "v_mov_b32_e32 %[a1], v68\n"
        "v_mov_b32_e32 %[b1], v70\n"
        "v_mov_b32_e32 %[a2], v72\n"
        "v_mov_b32_e32 %[b2], v74\n"
        "2:\n"
        : "=&{v[64:67]}"(t0), "=&{v[68:71]}"(t1), "=&{v[72:75]}"(t2),
          [a0] "=&v"(ga[0]), [a1] "=&v"(ga[1]), [a2] "=&v"(ga[2]), [b0] "=&v"(gb[0]), [b1] "=&v"(gb[1]), [b2] "=&v"(gb[2]), [bad] "=&v"(bad), [it] "=&s"(it)
        : [rw] "s"(rwd), [so0] "s"(so[0]), [so1] "s"(so[1]), [so2] "s"(so[2]), [off] "v"(vp[0]), [tag] "v"(tag), [slp] "n"(X2SLP)
        : "vcc", "scc", "memory");
    const f32x2 own = *(const __attribute__((address_space(3))) f32x2*)(size_t)a_own;
    float mj[3], lj[3];
#pragma unroll
    for (int j = 0; j < 3; ++j) { mj[j] = as_f(__builtin_amdgcn_readlane(ga[j], 24)); lj[j] = as_f(__builtin_amdgcn_readlane(gb[j], 24)); }
    const float MM = fmaxf(fmaxf(mj[0], mj[1]), fmaxf(mj[2], Ms));
    const float w3 = __expf(Ms - MM);
    float wj[3], LL = w3 * Ls;
#pragma unroll
    for (int j = 0; j < 3; ++j) { wj[j] = __expf(mj[j] - MM); LL += wj[j] * lj[j]; }
    float o0 = w3 * own[0], o1 = w3 * own[1];
#pragma unroll
    for (int j = 0; j < 3; ++j) { o0 += wj[j] * as_f(ga[j]); o1 += wj[j] * as_f(gb[j]); }
    const float inv = __builtin_amdgcn_rcpf(LL);
    o0 *= inv; o1 *= inv;
    ((lds_u32*)(size_t)a_o32)[0] = pk_bf16(o0, o0);
    ((lds_u32*)(size_t)a_o32)[8] = pk_bf16(o1, o1);
    asm volatile("" ::: "memory");
    *(volatile lds_u32*)(size_t)a_flag = 1u;
    asm volatile("s_waitcnt vmcnt(0)" ::"{v[64:67]}"(t0), "{v[68:71]}"(t1), "{v[72:75]}"(t2) : "memory");
  }
}

__global__ __launch_bounds__(64 * (NSW + 1), 3) void fused_attn_decode(Params p) {
  __shared__ __attribute__((aligned(16))) Smem sm;
  const int lane = threadIdx.x & 63, w = __builtin_amdgcn_readfirstlane(threadIdx.x >> 6);
  const int bid = blockIdx.x, x = bid & 7, i = bid >> 3;
  const int pair = x * 8 + (i >> 2), s = i & 3, b = pair >> 5, h = pair & 31;
  if (w == NSW) {
    if (lane < 8) (&sm.cnt_q)[lane] = 0;
    wait_lgkm0();
    __builtin_amdgcn_s_barrier();
    comm_wave(p, sm, b, h, s, lane);
  } else if (w < 2) {
    stream_wave<4>(p, sm, b, h, s, w, lane);
  } else {
    stream_wave<3>(p, sm, b, h, s, w, lane);
  }
}

extern "C" void kernel_launch(void* const* d_in, const int* in_sizes, int n_in, void* d_out, int out_size, void* d_ws,
                              size_t ws_size, hipStream_t stream) {
  Params p;
  for (int b = 0; b < 2; ++b) {
    p.x[b] = d_in[5 * b + 0]; p.k[b] = d_in[5 * b + 1]; p.v[b] = d_in[5 * b + 2];
    p.wq[b] = d_in[5 * b + 3]; p.wo[b] = d_in[5 * b + 4];
  }
  p.out = d_out; p.ws = d_ws;
  fused_attn_decode<<<256, 64 * (NSW + 1), 0, stream>>>(p);
}
```
